```python
import torch

def _softmax(x, axis=-1):
    return torch.softmax(x, dim=axis)

def _gelu(x):
    return torch.nn.functional.gelu(x, approximate='tanh')

def _silu(x):
    return torch.nn.functional.silu(x)

def _sigmoid(x):
    return torch.sigmoid(x)

def _layernorm(x, axis=-1, eps=1e-05):
    m = x.mean(dim=axis, keepdim=True)
    v = x.var(dim=axis, keepdim=True, unbiased=False)
    return (x - m) / torch.sqrt(v + eps)

def _scatter_add(tgt, idx, src, axis=0):
    return tgt.clone().index_add_(axis, idx, src)

def _segment_sum(src, seg, n):
    out = torch.zeros((n,) + src.shape[1:], dtype=src.dtype)
    return out.index_add_(0, seg, src)

def _pad(x, pw):
    flat = tuple((v for pair in reversed(pw) for v in pair))
    return torch.nn.functional.pad(x, flat)
kernel_name = 'adaLN_cond__gqa_block_cached__mixer_token_mlp__decode'

def setup_inputs():

    _w = lambda a: (lambda t: t.to(torch.bfloat16) if t.is_floating_point() else t)(torch.as_tensor(a))

    def _A_setup():
        B, Sq, D = (1, 1, 1024)
        Hq, Hkv, Sk, Dh = (32, 4, 65536, 128)
        assert Hq % Hkv == 0, (Hq, Hkv)
        return {'x': _w((lambda *s: torch.randn(*s, dtype=torch.bfloat16) * 0.1)(B, Sq, D)), 'k_cache': _w((lambda *s: torch.randn(*s, dtype=torch.bfloat16) * 3.2)(B, Hkv, Sk, Dh)), 'v_cache': _w((lambda *s: torch.randn(*s, dtype=torch.bfloat16) * 1.6)(B, Hkv, Sk, Dh)), 'wq': _w((lambda *s: torch.randn(*s, dtype=torch.bfloat16) * 3.2)(D, Hq * Dh) * D ** (-0.5)), 'wo': _w((lambda *s: torch.randn(*s, dtype=torch.bfloat16) * 1.6)(Hq * Dh, D) * (Hq * Dh) ** (-0.5))}
    _A_in = _A_setup()

    def _B_setup():
        B, S, D, F = (1, 1, 1024, 3584)
        return {'x': _w((lambda *s: torch.randn(*s, dtype=torch.bfloat16) * 0.1)(B, S, D)), 'w1': _w((lambda *s: torch.randn(*s, dtype=torch.bfloat16) * 1.6)(S, F) * S ** (-0.5)), 'b1': _w((lambda *s: torch.randn(*s, dtype=torch.bfloat16) * 1.6)(F) * 0.02), 'w2': _w((lambda *s: torch.randn(*s, dtype=torch.bfloat16) * 1.6)(F, S) * F ** (-0.5)), 'b2': _w((lambda *s: torch.randn(*s, dtype=torch.bfloat16) * 1.6)(S) * 0.02)}
    _B_in = _B_setup()
    return {'A_x': _A_in['x'], 'A_k_cache': _A_in['k_cache'], 'A_v_cache': _A_in['v_cache'], 'A_wq': _A_in['wq'], 'A_wo': _A_in['wo'], 'B_x': _B_in['x'], 'B_w1': _B_in['w1'], 'B_b1': _B_in['b1'], 'B_w2': _B_in['w2'], 'B_b2': _B_in['b2'], 'c': _w((lambda *s: torch.randn(*s, dtype=torch.bfloat16) * 0.1)(1, 1024) * 8.0), 'w_mod': _w((lambda *s: torch.randn(*s, dtype=torch.bfloat16) * 0.1)(1024, 6 * 1024) * 1024 ** (-0.5) * 8.0)}

def reference(A_x, A_k_cache, A_v_cache, A_wq, A_wo, B_x, B_w1, B_b1, B_w2, B_b2, c, w_mod):

    def _A_ref(x, k_cache, v_cache, wq, wo):
        B, Sq, D = x.shape
        Hq, Dh = (32, 128)
        Hkv = k_cache.shape[1]
        rep = Hq // Hkv
        q = (lambda x, *d: x.permute(*d))((x @ wq).reshape(B, Sq, Hq, Dh), 0, 2, 1, 3)
        k = (lambda x, n, axis: torch.repeat_interleave(x, n, dim=axis))(k_cache, rep, axis=1)
        v = (lambda x, n, axis: torch.repeat_interleave(x, n, dim=axis))(v_cache, rep, axis=1)
        scores = torch.einsum('bhqd,bhkd->bhqk', q, k) * Dh ** (-0.5)
        p = _softmax(scores, axis=-1)
        o = torch.einsum('bhqk,bhkd->bhqd', p, v)
        return (lambda x, *d: x.permute(*d))(o, 0, 2, 1, 3).reshape(B, Sq, Hq * Dh) @ wo

    def _B_ref(x, w1, b1, w2, b2):
        B, S, D = x.shape
        h = _layernorm(x, axis=-1)
        u = torch.einsum('bsd,sf->bdf', h, w1) + b1
        u = _gelu(u)
        y = torch.einsum('bdf,fs->bds', u, w2) + b2
        return (lambda x, *d: x.permute(*d))(y, 0, 2, 1)
    x = A_x
    mod = (_silu(c) @ w_mod).reshape(c.shape[0], 6, 1, -1)
    sa, ca, ga, sb, cb, gb = (mod[:, 0].clone(), mod[:, 1].clone(), mod[:, 2].clone(), mod[:, 3].clone(), mod[:, 4].clone(), mod[:, 5].clone())
    h = _layernorm(x, axis=-1) * (1 + ca) + sa
    x = x + ga * _A_ref(h, A_k_cache, A_v_cache, A_wq, A_wo)
    h = _layernorm(x, axis=-1) * (1 + cb) + sb
    x = x + gb * _B_ref(h, B_w1, B_b1, B_w2, B_b2)
    return x
```

```cpp
#include <hip/hip_runtime.h>
#include <hip/hip_bf16.h>
#include <stdint.h>
#define MIMIC 1
typedef unsigned short u16;
typedef unsigned v4u __attribute__((ext_vector_type(4)));
constexpr int D = 1024, HQ = 32, HKV = 4, SK = 65536, DH = 128, F = 3584;
constexpr int NSPLIT = 256;
constexpr int KPS = SK / NSPLIT;

__device__ __forceinline__ float bf2f(u16 b) { return __uint_as_float(((unsigned)b) << 16); }
__device__ __forceinline__ float rbf(float f) {
  return __bfloat162float(__float2bfloat16(f));
}
__device__ __forceinline__ float rbf_always(float f) { return __bfloat162float(__float2bfloat16(f)); }

__device__ float block_sum(float v, float* red) {
  for (int o = 32; o > 0; o >>= 1) v += __shfl_xor(v, o);
  int w = threadIdx.x >> 6, nw = blockDim.x >> 6;
  __syncthreads();
  if ((threadIdx.x & 63) == 0) red[w] = v;
  __syncthreads();
  float s = 0;
  for (int i = 0; i < nw; ++i) s += red[i];
  return s;
}

__global__ void k_mod(const u16* c, const u16* wmod, float* mod) {
  __shared__ float sc[D];
  __shared__ float red[32][64];
  for (int i = threadIdx.x; i < D; i += 256) { float v = bf2f(c[i]); sc[i] = rbf(v / (1.f + __expf(-v))); }
  __syncthreads();
  int cc = threadIdx.x & 7, rg = threadIdx.x >> 3;
  int col0 = blockIdx.x * 64 + cc * 8;
  float acc[8] = {0};
  for (int r = rg; r < D; r += 32) {
    v4u w = *(const v4u*)(wmod + (size_t)r * 6144 + col0);
    float s = sc[r];
    for (int j = 0; j < 4; ++j) { acc[2*j] += s * __uint_as_float(w[j] << 16); acc[2*j+1] += s * __uint_as_float(w[j] & 0xffff0000u); }
  }
  for (int j = 0; j < 8; ++j) red[rg][cc * 8 + j] = acc[j];
  __syncthreads();
  if (threadIdx.x < 64) { float s = 0; for (int i = 0; i < 32; ++i) s += red[i][threadIdx.x]; mod[blockIdx.x * 64 + threadIdx.x] = rbf(s); }
}

__device__ void layernorm_mimic(float* v  , float* red) {
  float s = 0; for (int i = threadIdx.x; i < D; i += blockDim.x) s += v[i];
  float mean = block_sum(s, red) / D;
  float s2 = 0; for (int i = threadIdx.x; i < D; i += blockDim.x) { float d = v[i] - mean; s2 += d * d; }
  float var = block_sum(s2, red) / D;
  float mb = rbf(mean), vb = rbf(var);
  float sd = rbf(sqrtf(rbf(vb + 1e-5f)));
  for (int i = threadIdx.x; i < D; i += blockDim.x) v[i] = rbf(rbf(v[i] - mb) / sd);
  __syncthreads();
}

__global__ void k_q(const u16* x, const float* mod, const u16* wq, float* q) {
  __shared__ float h[D];
  __shared__ float red[32][64];
  for (int i = threadIdx.x; i < D; i += 256) h[i] = bf2f(x[i]);
  __syncthreads();
  layernorm_mimic(h, &red[0][0]);
  for (int i = threadIdx.x; i < D; i += 256) h[i] = rbf(rbf(h[i] * rbf(1.f + mod[D + i])) + mod[i]);
  __syncthreads();
  int cc = threadIdx.x & 7, rg = threadIdx.x >> 3;
  int col0 = blockIdx.x * 64 + cc * 8;
  float acc[8] = {0};
  for (int r = rg; r < D; r += 32) {
    v4u w = *(const v4u*)(wq + (size_t)r * 4096 + col0);
    float s = h[r];
    for (int j = 0; j < 4; ++j) { acc[2*j] += s * __uint_as_float(w[j] << 16); acc[2*j+1] += s * __uint_as_float(w[j] & 0xffff0000u); }
  }
  for (int j = 0; j < 8; ++j) red[rg][cc * 8 + j] = acc[j];
  __syncthreads();
  if (threadIdx.x < 64) { float s = 0; for (int i = 0; i < 32; ++i) s += red[i][threadIdx.x]; q[blockIdx.x * 64 + threadIdx.x] = rbf(s); }
}

__global__ void k_attn(const float* q, const u16* K, const u16* V, float* pm, float* pl, float* po) {
  __shared__ float sq[8][DH];
  __shared__ float sp[8][KPS];
  __shared__ float red[8];
  __shared__ float so[16][8][DH];
  int split = blockIdx.x, kvh = blockIdx.y, t = threadIdx.x;
  for (int i = t; i < 8 * DH; i += 256) sq[i / DH][i % DH] = q[kvh * 8 * DH + i];
  __syncthreads();
  const u16* kr = K + ((size_t)kvh * SK + split * KPS + t) * DH;
  float s[8] = {0};
  for (int d0 = 0; d0 < DH; d0 += 8) {
    v4u w = *(const v4u*)(kr + d0);
    for (int j = 0; j < 4; ++j) {
      float a = __uint_as_float(w[j] << 16), b = __uint_as_float(w[j] & 0xffff0000u);
      for (int g = 0; g < 8; ++g) s[g] += a * sq[g][d0 + 2*j] + b * sq[g][d0 + 2*j + 1];
    }
  }
  float mx[8], sm[8];
  for (int g = 0; g < 8; ++g) {
    s[g] = rbf(rbf(s[g]) * 0.08838834764831845f);
    float m = s[g];
    for (int o = 32; o > 0; o >>= 1) m = fmaxf(m, __shfl_xor(m, o));
    __syncthreads();
    if ((t & 63) == 0) red[t >> 6] = m;
    __syncthreads();
    m = fmaxf(fmaxf(red[0], red[1]), fmaxf(red[2], red[3]));
    mx[g] = m;
    float p = __expf(s[g] - m);
    sm[g] = block_sum(p, red);
    sp[g][t] = p;
  }
  __syncthreads();
  int kg = t >> 4, c = t & 15;
  float acc[8][8] = {{0}};
  for (int k = kg; k < KPS; k += 16) {
    v4u w = *(const v4u*)(V + ((size_t)kvh * SK + split * KPS + k) * DH + c * 8);
    float vf[8];
    for (int j = 0; j < 4; ++j) { vf[2*j] = __uint_as_float(w[j] << 16); vf[2*j+1] = __uint_as_float(w[j] & 0xffff0000u); }
    for (int g = 0; g < 8; ++g) { float p = sp[g][k]; for (int j = 0; j < 8; ++j) acc[g][j] += p * vf[j]; }
  }
  for (int g = 0; g < 8; ++g) for (int j = 0; j < 8; ++j) so[kg][g][c * 8 + j] = acc[g][j];
  __syncthreads();
  for (int i = t; i < 8 * DH; i += 256) {
    int g = i / DH, d = i % DH; float a = 0;
    for (int k = 0; k < 16; ++k) a += so[k][g][d];
    po[((size_t)(kvh * 8 + g) * NSPLIT + split) * DH + d] = a;
  }
  if (t < 8) { pm[(kvh * 8 + t) * NSPLIT + split] = mx[t]; pl[(kvh * 8 + t) * NSPLIT + split] = sm[t]; }
}

__global__ void k_comb(const float* pm, const float* pl, const float* po, float* o) {
  int h = blockIdx.x, d = threadIdx.x;
  float M = -INFINITY;
  for (int s = 0; s < NSPLIT; ++s) M = fmaxf(M, pm[h * NSPLIT + s]);
  float L = 0, a = 0;
  for (int s = 0; s < NSPLIT; ++s) { float w = __expf(pm[h * NSPLIT + s] - M); L += w * pl[h * NSPLIT + s]; a += w * po[((size_t)h * NSPLIT + s) * DH + d]; }
  o[h * DH + d] = rbf(a / L);
}

__global__ void k_wo(const float* o, const u16* wo, float* part) {
  __shared__ float red[32][64];
  int cc = threadIdx.x & 7, rg = threadIdx.x >> 3;
  int col0 = blockIdx.x * 64 + cc * 8, r0 = blockIdx.y * 256;
  float acc[8] = {0};
  for (int r = r0 + rg; r < r0 + 256; r += 32) {
    v4u w = *(const v4u*)(wo + (size_t)r * 1024 + col0);
    float s = o[r];
    for (int j = 0; j < 4; ++j) { acc[2*j] += s * __uint_as_float(w[j] << 16); acc[2*j+1] += s * __uint_as_float(w[j] & 0xffff0000u); }
  }
  for (int j = 0; j < 8; ++j) red[rg][cc * 8 + j] = acc[j];
  __syncthreads();
  if (threadIdx.x < 64) { float s = 0; for (int i = 0; i < 32; ++i) s += red[i][threadIdx.x]; part[blockIdx.y * 1024 + blockIdx.x * 64 + threadIdx.x] = s; }
}

__global__ void k_fin(const u16* x, const float* mod, const float* part, const u16* w1, const u16* b1, const u16* w2, const u16* b2, __hip_bfloat16* out) {
  __shared__ float x1[D], h[D], red[8];
  for (int i = threadIdx.x; i < D; i += 256) {
    float a = 0; for (int k = 0; k < 16; ++k) a += part[k * 1024 + i];
    a = rbf(a);
    float v = rbf(bf2f(x[i]) + rbf(mod[2 * D + i] * a));
    x1[i] = v; h[i] = v;
  }
  __syncthreads();
  layernorm_mimic(h, red);
  for (int i = threadIdx.x; i < D; i += 256) h[i] = rbf(rbf(h[i] * rbf(1.f + mod[4 * D + i])) + mod[3 * D + i]);
  __syncthreads();
  layernorm_mimic(h, red);
  int w = threadIdx.x >> 6, lane = threadIdx.x & 63, d = blockIdx.x * 4 + w;
  float t = h[d], acc = 0;
  for (int f = lane; f < F; f += 64) {
    float u = rbf(rbf(t * bf2f(w1[f])) + bf2f(b1[f]));
    float z = 0.7978845608028654f * (u + 0.044715f * u * u * u);
    float g = rbf(0.5f * u * (1.f + tanhf(z)));
    acc += g * bf2f(w2[f]);
  }
  for (int o = 32; o > 0; o >>= 1) acc += __shfl_xor(acc, o);
  if (lane == 0) {
    float y = rbf(rbf(acc) + bf2f(b2[0]));
    out[d] = __float2bfloat16(x1[d] + rbf(mod[5 * D + d] * y));
  }
}

extern "C" void kernel_launch(void* const* d_in, const int* in_sizes, int n_in, void* d_out, int out_size, void* d_ws, size_t ws_size, hipStream_t stream) {
  const u16 *x = (const u16*)d_in[0], *K = (const u16*)d_in[1], *V = (const u16*)d_in[2], *wq = (const u16*)d_in[3], *wo = (const u16*)d_in[4];
  const u16 *w1 = (const u16*)d_in[6], *b1 = (const u16*)d_in[7], *w2 = (const u16*)d_in[8], *b2 = (const u16*)d_in[9], *c = (const u16*)d_in[10], *wmod = (const u16*)d_in[11];
  float* ws = (float*)d_ws;
  float *mod = ws, *q = ws + 8192, *o = ws + 16384, *part = ws + 32768, *pm = ws + 65536, *pl = pm + 32 * NSPLIT, *po = ws + 131072;
  k_mod<<<96, 256, 0, stream>>>(c, wmod, mod);
  k_q<<<64, 256, 0, stream>>>(x, mod, wq, q);
  k_attn<<<dim3(NSPLIT, HKV), 256, 0, stream>>>(q, K, V, pm, pl, po);
  k_comb<<<32, 128, 0, stream>>>(pm, pl, po, o);
  k_wo<<<dim3(16, 16), 256, 0, stream>>>(o, wo, part);
  k_fin<<<256, 256, 0, stream>>>(x, mod, part, w1, b1, w2, b2, (__hip_bfloat16*)d_out);
}
```

```cpp
#include <hip/hip_runtime.h>
#include <hip/hip_bf16.h>
#include <stdint.h>

#define STAMPS 0
#define TAU 3500
#define LEAD -8
#define TWM 55
#define TTS 165
#define TH 385
#define TQ 935
#define TMP 1135
#define TMLB 170
#define WOD 15
#define TA 140
#define DLA 50
#define DLB 40
#define DLC 150
#define TABLE 1
#define XM 4
#define TE 2
#define TEXP 114
constexpr int WOE = 1;
constexpr int VPRE = 7;
constexpr int PVL = 5;
constexpr int k_tick(int s, int n) { return (int)((long long)((80 - LEAD) * n + 256 * s) * TAU / (1000 * n)); }
constexpr int v_tick(int s, int n) { return (int)((long long)((336 - LEAD) * n + (256 + 8 * WOE) * s) * TAU / (1000 * n)); }
constexpr int T_END = v_tick(1, 1);

typedef unsigned u32;
typedef unsigned short u16;
typedef unsigned long long u64;
typedef u32 v4u __attribute__((ext_vector_type(4)));
typedef u32 v2u __attribute__((ext_vector_type(2)));
typedef float v4f __attribute__((ext_vector_type(4)));
typedef float v2f __attribute__((ext_vector_type(2)));
typedef __bf16 bf16x8 __attribute__((ext_vector_type(8)));
typedef __bf16 bf16x2 __attribute__((ext_vector_type(2)));
typedef __amdgpu_buffer_rsrc_t rsrc_t;
#define DEV __device__ __forceinline__
#define LAS __attribute__((address_space(3)))

constexpr int sub_time(int w, int k) { return k * (w < 4 ? 256 : 320) + w * 10; }
constexpr u64 ranks(int w) {
  u64 t = 0;
  for (int k = 0; k < (w < 4 ? 10 : 8); ++k) {
    int n = 0;
    for (int v = 0; v < 7; ++v)
      for (int i = 0; i < (v < 4 ? 10 : 8); ++i) n += sub_time(v, i) < sub_time(w, k) || (sub_time(v, i) == sub_time(w, k) && v < w);
    t |= (u64)n << (6 * k);
  }
  return t;
}

constexpr int D = 1024, FF = 3584;
constexpr int NT_ = 2, SC1 = 16;
constexpr int OOB = 0x7ffffff0;
constexpr int SPIN_CAP = 1 << 16;
constexpr u32 TB = 7u;
constexpr float LOG2E = 1.4426950408889634f;

constexpr int WS_EPOCH = 0;
constexpr int WS_C = 4096;
constexpr int WS_TS = 16384;
constexpr int WS_ML = 32768;
constexpr int WS_H = 65536;
constexpr int WS_Q = WS_H + 65536;
constexpr int WS_MP = WS_Q + 65536;
constexpr int WS_MR = WS_MP + 65536;
constexpr int WS_TAB = WS_MR + 16384;
constexpr int WS_B = 524288;
constexpr int WS_A = WS_B + 524288;
constexpr int WS_END = WS_A + 1048576;
constexpr int WS_STAMP = 4 << 20;

constexpr int L_WV = 0;
constexpr int L_Q = 28672;
constexpr int L_VEC = L_Q + 2048;
constexpr int L_H = L_VEC + 2048;
constexpr int L_MR = L_H + 512;
constexpr int L_ML = L_MR + 8192 + 256;
constexpr int L_BM = L_ML + 512;
constexpr int L_RM = L_BM + 64;
constexpr int L_RA = L_RM + 256;
constexpr int L_RB = L_RA + 1024;
constexpr int L_S1 = L_RB + 1024;
constexpr int L_S2 = L_S1 + 128;
constexpr int L_X = L_S2 + 128;
constexpr int L_RF = L_X + 64;
constexpr int L_TB = L_RF + 128;
constexpr int L_FLAG = L_TB + 512;
constexpr int L_WT = L_FLAG + 64;
constexpr int L_MLP = L_WT + 2 * 34816;
constexpr int L_END = L_MLP + 3 * 7168 + 2048 + 16;
constexpr int F_Q = L_FLAG, F_SM = L_FLAG + 4, F_BM = L_FLAG + 8, F_ORG = L_FLAG + 12, F_WQ = L_FLAG + 16, F_WM = L_FLAG + 20, F_TAG = L_FLAG + 24, F_TB = L_FLAG + 28;

template <int I> struct IC { static constexpr int v = I; };
template <int I, int N, class F> DEV void static_for(F&& f) { if constexpr (I < N) { f(IC<I>{}); static_for<I + 1, N>(f); } }
template <class T, class S> DEV T bc(S s) { return __builtin_bit_cast(T, s); }
DEV u32 pk2(float lo, float hi) { u32 r = bc<u32>(__builtin_convertvector(v2f{lo, hi}, bf16x2)); asm("" : "+v"(r)); return r; }
DEV float lo16(u32 v) { return bc<float>(v << 16); }
DEV float hi16(u32 v) { return bc<float>(v & 0xffff0000u); }
DEV float rbf(float x) { return lo16(pk2(x, x)); }
DEV float rbq(float x) { return (float)(__bf16)x; }
DEV v2f unpk(u32 v) { return v2f{lo16(v), hi16(v)}; }
DEV v2f rbf2(v2f v) { return unpk(pk2(v.x, v.y)); }
DEV float bf2f(u16 v) { return bc<float>((u32)v << 16); }
DEV float dot2(u32 a, u32 b, float c) { return __builtin_amdgcn_fdot2_f32_bf16(bc<bf16x2>(a), bc<bf16x2>(b), c, false); }
DEV v4f mfma(v4u a, v4u b, v4f c) { return __builtin_amdgcn_mfma_f32_16x16x32_bf16(bc<bf16x8>(a), bc<bf16x8>(b), c, 0, 0, 0); }
DEV u32 permlo(u32 r0, u32 r1) { return __builtin_amdgcn_perm(r1, r0, 0x05040100u); }
DEV u32 permhi(u32 r0, u32 r1) { return __builtin_amdgcn_perm(r1, r0, 0x07060302u); }
DEV float ex2(float x) { return __builtin_amdgcn_exp2f(x); }
DEV float rcp(float x) { return __builtin_amdgcn_rcpf(x); }
template <int CTRL> DEV float dpp(float x) { return bc<float>(__builtin_amdgcn_mov_dpp(bc<int>(x), CTRL, 0xf, 0xf, true)); }
constexpr int XOR1 = 0xB1, XOR2 = 0x4E, XOR7 = 0x141, XOR8 = 0x128, ROR4 = 0x124;
DEV float tr32(float a, float b) { auto s = __builtin_amdgcn_permlane32_swap(bc<u32>(a), bc<u32>(b), false, false); return bc<float>(s[0]) + bc<float>(s[1]); }
DEV float tr16(float a, float b) { auto s = __builtin_amdgcn_permlane16_swap(bc<u32>(a), bc<u32>(b), false, false); return bc<float>(s[0]) + bc<float>(s[1]); }
DEV float tr8(float a, float b, bool b3) { const float t1 = a + dpp<XOR8>(a), t2 = b + dpp<XOR8>(b); return b3 ? t2 : t1; }
DEV float sum32(float a) { return tr32(a, a); }
DEV float sum16(float a) { return tr16(a, a); }
DEV float sum8(float a) { return a + dpp<XOR8>(a); }
DEV float sum421(float a) { a += dpp<XOR1>(a); a += dpp<XOR2>(a); return a + dpp<XOR7>(a); }
DEV float sum345(float a) { return sum32(sum16(sum8(a))); }
DEV float wsum(float a) { return sum345(sum421(a)); }
DEV float max32(float a) { auto s = __builtin_amdgcn_permlane32_swap(bc<u32>(a), bc<u32>(a), false, false); return fmaxf(bc<float>(s[0]), bc<float>(s[1])); }
DEV float max16(float a) { auto s = __builtin_amdgcn_permlane16_swap(bc<u32>(a), bc<u32>(a), false, false); return fmaxf(bc<float>(s[0]), bc<float>(s[1])); }
DEV float max345(float a) { return max32(max16(fmaxf(a, dpp<XOR8>(a)))); }
DEV float lane_of(float v, int l) { return bc<float>(__builtin_amdgcn_readlane(bc<int>(v), l)); }
DEV float tr8cols(const float (&a)[8], int lane) { return tr8(tr16(tr32(a[0], a[4]), tr32(a[2], a[6])), tr16(tr32(a[1], a[5]), tr32(a[3], a[7])), lane & 8); }

template <class T> DEV T sel(bool c, T a, T b) {
  if (!__builtin_constant_p(a)) asm("" : "+s"(a));
  if (!__builtin_constant_p(b)) asm("" : "+s"(b));
  return c ? a : b;
}
DEV rsrc_t mk(const void* p, u32 bytes) { return __builtin_amdgcn_make_buffer_rsrc((void*)p, 0, bytes, 0x00020000); }
template <int AUX> DEV v4u ld16(rsrc_t r, int voff, int soff = 0) { return __builtin_amdgcn_raw_buffer_load_b128(r, voff, soff, AUX); }
template <int AUX> DEV u32 ld4(rsrc_t r, int voff, int soff = 0) { return __builtin_amdgcn_raw_buffer_load_b32(r, voff, soff, AUX); }
DEV u16 ld2(rsrc_t r, int voff) { return __builtin_amdgcn_raw_buffer_load_b16(r, voff, 0, 0); }
DEV void st16(rsrc_t r, int voff, v4u v) { __builtin_amdgcn_raw_buffer_store_b128(v, r, voff, 0, SC1); }
DEV void st4(rsrc_t r, int voff, u32 v) { __builtin_amdgcn_raw_buffer_store_b32(v, r, voff, 0, SC1); }

#define MEMB() asm volatile("" ::: "memory")
#define SCHED() __builtin_amdgcn_sched_barrier(0)
#define PRIO(n) do { SCHED(); __builtin_amdgcn_s_setprio(n); SCHED(); } while (0)
#define WAIT_LDS() asm volatile("s_waitcnt lgkmcnt(0)" ::: "memory")
template <int N> DEV void pace() { asm volatile("s_waitcnt vmcnt(%0)" ::"n"(N) : "memory"); }
DEV void bar() { WAIT_LDS(); __builtin_amdgcn_s_barrier(); MEMB(); }
DEV u32 now() { return (u32)__builtin_amdgcn_s_memrealtime(); }
DEV void wait_until(u32 t) {
#pragma nounroll
  while ((int)(now() - t) < 0) __builtin_amdgcn_s_sleep(1);
  MEMB();
}
DEV u32 lds_word(LAS unsigned char* L, int off) { return __builtin_amdgcn_readfirstlane(*(volatile LAS u32*)(L + off)); }
DEV void lds_wait(LAS unsigned char* L, int off, u32 want) {
#pragma nounroll
  for (int s = 0; s < (SPIN_CAP << 4) && lds_word(L, off) < want; ++s) __builtin_amdgcn_s_sleep(2);
  MEMB();
}
DEV void lds_count(LAS unsigned char* L, int off, int lane) {
  WAIT_LDS();
  if (lane == 0) __hip_atomic_fetch_add((LAS u32*)(L + off), 1u, __ATOMIC_RELAXED, __HIP_MEMORY_SCOPE_WORKGROUP);
}

DEV u32 tagged(float v, u32 tag) { return ((bc<u32>(v) + (TB + 1) / 2) & ~TB) | tag; }
DEV u32 cut(float v, u32 tag) { return (bc<u32>(v) & ~TB) | tag; }
DEV v4u cut4(v4f v, u32 tag) { return (bc<v4u>(v) & ~TB) | tag; }
DEV float untag(u32 v) { return bc<float>(v & ~TB); }
DEV v4f untag4(v4u v) { return bc<v4f>(v & ~TB); }
DEV u32 mism(u32 v, u32 tag) { return v ^ tag; }
DEV u32 mism(v4u v, u32 tag) { return ((v.x ^ v.y) | (v.x ^ v.z)) | ((v.x ^ v.w) | (v.x ^ tag)); }
template <int SLEEP, class T, int N, class F> DEV void gather(T (&v)[N], u32 tag, F load) {
#pragma nounroll
  for (int s = 0; s < SPIN_CAP; ++s) {
    u32 bad = 0;
#pragma unroll
    for (int i = 0; i < N; ++i) bad |= mism(v[i], tag);
    if (__all((bad & TB) == 0)) break;
    if (SLEEP) __builtin_amdgcn_s_sleep(SLEEP);
    MEMB();
    load();
  }
  MEMB();
}

struct Params {
  const void *x, *k, *v, *wq, *wo, *w1, *b1, *w2, *b2, *c, *wmod;
  void *out, *ws;
};

#define STAMP(i) do { } while (0)

DEV void mlp_step(v2f& acc, v2f t, float w1, float b1, float w2) {
  constexpr float K0 = -2.f * 0.7978845608028654f * LOG2E, K1 = K0 * 0.044715f;
  const v2f u = t * w1 + b1, z = u * ((u * u) * K1 + K0), d = v2f{ex2(z.x), ex2(z.y)} + 1.f;
  acc += (u * w2) * v2f{rcp(d.x), rcp(d.y)};
}
DEV v2f tab_input(int b, int q) {
  const u32 bits = ((b >> 7) << 15) + (TEXP << 7) + (b & 127) * 16 + 2 * q;
  return v2f{q == 0 && b == 0 ? 0.f : bc<float>(bits << 16), bc<float>((bits + 1) << 16)};
}
DEV void tab_write(LAS unsigned char* L, const v2f (&a)[8], int wave, int lane) {
  v2f r;
  r.x = sum421(tr8(tr16(tr32(a[0].x, a[4].x), tr32(a[2].x, a[6].x)), tr16(tr32(a[1].x, a[5].x), tr32(a[3].x, a[7].x)), lane & 8));
  r.y = sum421(tr8(tr16(tr32(a[0].y, a[4].y), tr32(a[2].y, a[6].y)), tr16(tr32(a[1].y, a[5].y), tr32(a[3].y, a[7].y)), lane & 8));
  *(LAS v2f*)(L + L_TB + wave * 64 + (lane >> 3) * 8) = r;
  lds_count(L, F_TB, lane);
}

DEV v4u load_wo(const Params& p, int prow, int half, int wave, int lane, int i) {
  return ld16<NT_>(mk(p.wo, 4096 * 1024 * 2), (lane >> 3) * 2048 + (lane & 7) * 16, prow * 32 * 2048 + half * 1024 + wave * 128 + (i ^ (prow & 1)) * 8 * 2048);
}
DEV void issue_wo(const Params& p, int prow, int half, int wave, int lane, v4u (&w)[4], u32 org) {
  wait_until(org + T_END + WOD);
#pragma unroll
  for (int i = WOE; i < 4; ++i) w[i] = load_wo(p, prow, half, wave, lane, i);
  SCHED();
}

template <int NT, int WIN>
DEV void stream_wave(const Params& p, u32 tstart, LAS unsigned char* L, int b, int kvh, int j, int wave, int lane, int prow, int half,
                     const v4u (&xr)[16], const v4u (&mv)[3], v4u (&wo)[4]) {
  constexpr int NS = 2 * NT, NK = 8 * NT, W0 = NT == 5 ? 0 : 4;
  const int ws_ = wave - 1, g = lane >> 4, c = lane & 15, voff = lane * 16;
  const rsrc_t rk = mk(p.k, 0x4000000u), rv = mk(p.v, 0x4000000u);
  constexpr u64 r0 = ranks(W0), r1 = ranks(W0 + 1), r2 = ranks(W0 + 2), r3 = ranks(3);
  const u64 rank = sel(ws_ == W0, r0, sel(ws_ == W0 + 1, r1, sel(ws_ == W0 + 2, r2, r3)));
  const int sb = (kvh * 4096 + j * 8) * 4096;
  auto soff = [&](int m) {
    const int r = ((int)(rank >> (6 * (m >> 2))) + 16 * kvh) & 63;
    return sb + (((r >> 3) << 9) + ((r + j + 2 * kvh) & 7)) * 4096 + ((m + (j >> 3) + kvh) & 3) * 1024;
  };
  LAS unsigned char* W = L + L_WV + ws_ * 4096;
  v4u kf[NS][4], vv[NT][8];
  const u32 stag = ws_ * TAU / 1000;
  u32 org = tstart + stag;

  auto gate = [&](int s, int tick) {
    if (s >= WIN) pace<WIN - 1>();
    if (s >= 8 && s < 24 && (s & 3) == 0) org = lds_word(L, F_ORG) + stag;
    wait_until(org + tick);
  };
  auto issueV = [&](auto M) {
    constexpr int m = M.v, x = m >= NK - 14;
    if constexpr (m == NK - 14) {
      gate(NK + m, v_tick(m, NK + 1));
      wo[0] = load_wo(p, prow, half, wave, lane, 0);
      SCHED();
    }
    gate(NK + m + x, v_tick(m + x, NK + 1));
    vv[m >> 3][m & 7] = ld16<NT_>(rv, voff, soff(m));
    SCHED();
  };
  auto bounce = [&](int n) {
#pragma unroll
    for (int i = 0; i < 4; ++i) { const int r = 4 * g + i; *(LAS v4u*)(W + r * 256 + ((c ^ r) * 16)) = kf[n][i]; }
#pragma unroll
    for (int s = 0; s < 4; ++s) kf[n][s] = *(const LAS v4u*)(W + c * 256 + (((4 * s + g) ^ c) * 16));
    SCHED();
  };
  auto copy_tiles = [&]() {
#pragma unroll
    for (int i = 0; i < 3; ++i) *(LAS v4u*)(L + L_MLP + i * 7168 + ws_ * 1024 + voff) = mv[i];
#pragma unroll
    for (int k = 0; k < (NT == 4 ? 6 : 4); ++k) {
      const int pi = NT == 5 ? 17 + 4 * ws_ + k : ws_ == 4 ? k : k == 5 ? 16 : 5 * ws_ - 19 + k;
      v4u lo, hi;
#pragma unroll
      for (int d = 0; d < 4; ++d) { lo[d] = permlo(xr[2 * k][d], xr[2 * k + 1][d]); hi[d] = permhi(xr[2 * k][d], xr[2 * k + 1][d]); }
      *(LAS v4u*)(L + L_WT + pi * 2048 + voff) = lo;
      *(LAS v4u*)(L + L_WT + pi * 2048 + 1024 + voff) = hi;
    }
    lds_count(L, NT == 4 ? F_WQ : F_WM, lane);
    SCHED();
  };

  v2f ta[8];
#pragma unroll
  for (int q = 0; q < 8; ++q) ta[q] = v2f{0.f, 0.f};
  auto table = [&](auto N) {
    constexpr int i = N.v / 4, q = 2 * (N.v % 4);
    const float w1 = unpk(mv[0][i >> 1])[i & 1], b1 = unpk(mv[1][i >> 1])[i & 1], w2 = unpk(mv[2][i >> 1])[i & 1];
    mlp_step(ta[q], tab_input(b, q), w1, b1, w2);
    mlp_step(ta[q + 1], tab_input(b, q + 1), w1, b1, w2);
    asm volatile("" : "+v"(ta[q]), "+v"(ta[q + 1]));
    SCHED();
  };
  constexpr int TN = 28, TC = NT == 5 ? XM : 1, T0 = TC + 1, TS = NK - TE - T0;
  static_for<0, NK>([&](auto S) {
    constexpr int m = S.v;
    gate(m, k_tick(m, NK));
    kf[m >> 2][m & 3] = ld16<NT_>(rk, voff, soff(m));
    SCHED();
    if constexpr ((m & 3) == 3 && m >= 11) bounce((m >> 2) - 2);
    if constexpr (m == TC) copy_tiles();
    if constexpr (TABLE && m >= T0 && m < T0 + TS) static_for<(m - T0) * TN / TS, (m - T0 + 1) * TN / TS>(table);
    if constexpr (TABLE && m == T0 + TS) { tab_write(L, ta, wave, lane); SCHED(); }
  });
  static_for<0, VPRE - 4>(issueV);
  bounce(NS - 2);
  static_for<VPRE - 4, VPRE>(issueV);
  bounce(NS - 1);
  STAMP(1);
  lds_wait(L, F_Q, 1);
  STAMP(2);
  v4u qf[4];
#pragma unroll
  for (int s = 0; s < 4; ++s) qf[s] = *(const LAS v4u*)(L + L_Q + (c & 7) * 256 + (4 * s + g) * 16);
  SCHED();
  issueV(IC<VPRE>{});

  float sc[NT][4];
  float mx = -INFINITY;
  v4f a0[NT], a1[NT];
  auto qk = [&](int t) {
    a0[t] = a1[t] = v4f{0.f, 0.f, 0.f, 0.f};
#pragma unroll
    for (int s = 0; s < 4; ++s) { a0[t] = mfma(kf[2 * t][s], qf[s], a0[t]); a1[t] = mfma(kf[2 * t + 1][s], qf[s], a1[t]); }
  };
  qk(0);
  SCHED();
  issueV(IC<VPRE + 1>{});
  static_for<0, NT>([&](auto T) {
    constexpr int t = T.v;
    if constexpr (t + 1 < NT) qk(t + 1);
    const v4f a = (lane & 8) ? a1[t] : a0[t];
#pragma unroll
    for (int h = 0; h < 2; ++h) {
      const v2f r = rbf2(rbf2(v2f{a[2 * h], a[2 * h + 1]}) * 0.08838834764831845f);
      sc[t][2 * h] = r.x; sc[t][2 * h + 1] = r.y;
      mx = fmaxf(mx, fmaxf(r.x, r.y));
    }
    SCHED();
    issueV(IC<VPRE + 2 + t>{});
  });
  const float mq = max345(mx) * LOG2E;
  float ls = 0.f;
  v4u pf[NT];
  static_for<0, NT>([&](auto T) {
    constexpr int t = T.v;
    float pp[4];
#pragma unroll
    for (int h = 0; h < 4; ++h) pp[h] = ex2(fmaf(sc[t][h], LOG2E, -mq));
    ls += (pp[0] + pp[1]) + (pp[2] + pp[3]);
    pf[t][0] = pk2(pp[0], pp[1]); pf[t][1] = pk2(pp[2], pp[3]);
    pf[t][2] = bc<u32>(dpp<XOR8>(bc<float>(pf[t][0]))); pf[t][3] = bc<u32>(dpp<XOR8>(bc<float>(pf[t][1])));
    SCHED();
    issueV(IC<VPRE + NT + 2 + t>{});
  });
  ls = sum345(ls);
  if (lane < 8) *(LAS v2f*)(L + L_ML + (ws_ * 8 + lane) * 8) = v2f{mq, ls};
  lds_count(L, F_SM, lane);
  STAMP(3);
  SCHED();

  v4f o[8];
#pragma unroll
  for (int e = 0; e < 8; ++e) o[e] = v4f{0.f, 0.f, 0.f, 0.f};
  v4u f[8];
  auto vperm = [&](int t, int jj) {
#pragma unroll
    for (int e = 0; e < 8; ++e)
      f[e][jj] = (e & 1) ? permhi(vv[t][2 * jj][e >> 1], vv[t][2 * jj + 1][e >> 1]) : permlo(vv[t][2 * jj][e >> 1], vv[t][2 * jj + 1][e >> 1]);
  };
  auto vmma = [&](int t) {
#pragma unroll
    for (int e = 0; e < 8; ++e) o[e] = mfma(f[e], pf[t], o[e]);
    SCHED();
  };
  float fac = 0.f;
  constexpr int done = VPRE + 2 * NT + 2;
  auto pv_at = [](int t) { const int n = 8 * (t + 1) + PVL, m = done + 2 * t; return n > m ? n : m; };
  static_for<0, NT - 1>([&](auto T) {
    constexpr int t = T.v;
    static_for<t == 0 ? done : pv_at(t - 1) + 1, pv_at(t)>(issueV);
    vperm(t, 0); vperm(t, 1); vperm(t, 2); vperm(t, 3);
#pragma unroll
    for (int e = 0; e < 8; ++e) asm volatile("" : "+v"(f[e]));
    SCHED();
    issueV(IC<pv_at(t)>{});
    vmma(t);
    if constexpr (t == NT - 3) {
      lds_wait(L, F_BM, 1);
      fac = ex2(mq - *(const LAS float*)(L + L_BM + (c & 7) * 4));
      asm volatile("" : "+v"(fac));
      SCHED();
    }
  });
  static_for<pv_at(NT - 2) + 1, NK>(issueV);
  STAMP(4);
  PRIO(2);
  issue_wo(p, prow, half, wave, lane, wo, org);
  vperm(NT - 1, 0); vperm(NT - 1, 1); vperm(NT - 1, 2);
  SCHED();
  vperm(NT - 1, 3);
  vmma(NT - 1);
  STAMP(5);
  LAS unsigned char* dst = W + ((c & 7) * 4 + g) * 128;
#pragma unroll
  for (int e = 0; e < 4; ++e) {
    v4f v;
#pragma unroll
    for (int r = 0; r < 4; ++r) v[r] = bc<float>(__builtin_amdgcn_update_dpp(bc<int>(o[e][r]), bc<int>(o[e + 4][r]), XOR8, 0xf, 0xc, false));
    *(LAS v4f*)(dst + (((e + (c >> 3) * 4) ^ (c & 7)) * 16)) = v * fac;
  }
  SCHED();
}

DEV float gemv_regs(const v4u (&w)[16], const LAS unsigned char* vec, int lane) {
  const v4u h0 = *(const LAS v4u*)(vec + (lane >> 3) * 32), h1 = *(const LAS v4u*)(vec + (lane >> 3) * 32 + 16);
  float a[8] = {0.f, 0.f, 0.f, 0.f, 0.f, 0.f, 0.f, 0.f};
#pragma unroll
  for (int k = 0; k < 8; ++k)
#pragma unroll
    for (int d = 0; d < 4; ++d) {
      const u32 h = k < 4 ? h0[k & 3] : h1[k & 3];
      a[2 * d] = dot2(permlo(w[2 * k][d], w[2 * k + 1][d]), h, a[2 * d]);
      a[2 * d + 1] = dot2(permhi(w[2 * k][d], w[2 * k + 1][d]), h, a[2 * d + 1]);
    }
  return tr8cols(a, lane);
}
DEV float gemv_lds(const LAS unsigned char* t, const LAS unsigned char* vec, int lane) {
  v4u hv[4];
#pragma unroll
  for (int i = 0; i < 4; ++i) hv[i] = *(const LAS v4u*)(vec + (lane >> 3) * 64 + i * 16);
  float a[8] = {0.f, 0.f, 0.f, 0.f, 0.f, 0.f, 0.f, 0.f};
#pragma unroll
  for (int k = 0; k < 16; ++k) {
    const v4u lo = *(const LAS v4u*)(t + k * 2048 + lane * 16), hi = *(const LAS v4u*)(t + k * 2048 + 1024 + lane * 16);
#pragma unroll
    for (int d = 0; d < 4; ++d) { a[2 * d] = dot2(lo[d], hv[k >> 2][k & 3], a[2 * d]); a[2 * d + 1] = dot2(hi[d], hv[k >> 2][k & 3], a[2 * d + 1]); }
  }
  return tr8cols(a, lane);
}

DEV void comm_wave(const Params& p, u32 tstart, LAS unsigned char* L, rsrc_t ws, int b, int x8, int i32, int kvh, int j, int lane, int prow, int half, int ci,
                   u32 epoch, const v4u (&wm)[16], const v4u (&sm)[4], u16 b2, v4u (&wo)[4]) {
  const int colb = ((lane & 7) * 8 + (lane >> 3)) * 4;
  const u32 tag = (epoch + 1) & TB;
  {
    v4u s[2];
#pragma unroll
    for (int k = 0; k < 8; ++k) {
      const v2f a = unpk(sm[k >> 2][k & 3]);
      s[k >> 2][k & 3] = pk2(a.x * rcp(1.f + ex2(-LOG2E * a.x)), a.y * rcp(1.f + ex2(-LOG2E * a.y)));
    }
    *(LAS v4u*)(L + L_VEC + lane * 32) = s[0];
    *(LAS v4u*)(L + L_VEC + lane * 32 + 16) = s[1];
    WAIT_LDS();
    STAMP(16);
    st4(ws, WS_H + x8 * 8192 + i32 * 256 + colb, tagged(gemv_regs(wm, L + L_VEC + x8 * 256, lane), tag));
  }
  STAMP(1);
  SCHED();
  float xn[4];
  {
    *(LAS v4u*)(L + L_MLP + 3 * 7168 + lane * 16) = sm[2];
    *(LAS v4u*)(L + L_MLP + 3 * 7168 + 1024 + lane * 16) = sm[3];
    *(LAS u16*)(L + L_MLP + 3 * 7168 + 2048) = b2;
    const v2u xm = *(const LAS v2u*)(L + L_MLP + 3 * 7168 + kvh * 512 + lane * 8);
    v2f xa[8], s = {0.f, 0.f}, q = {0.f, 0.f};
#pragma unroll
    for (int i = 0; i < 8; ++i) { xa[i] = unpk(sm[2 + (i >> 2)][i & 3]); s += xa[i]; }
    const float mean = wsum(s.x + s.y) * (1.f / D);
#pragma unroll
    for (int i = 0; i < 8; ++i) q += (xa[i] - mean) * (xa[i] - mean);
    const float mb = rbf(mean), sd = rbf(sqrtf(rbf(rbf(wsum(q.x + q.y) * (1.f / D)) + 1e-5f)));
    xn[0] = rbf(rbf(lo16(xm.x) - mb) / sd); xn[1] = rbf(rbf(hi16(xm.x) - mb) / sd); xn[2] = rbf(rbf(lo16(xm.y) - mb) / sd); xn[3] = rbf(rbf(hi16(xm.y) - mb) / sd);
  }
  SCHED();
  u32 torg, tmax;
  {
    wait_until(tstart + TTS);
    int dt = (int)(ld4<SC1>(ws, lane < 8 ? WS_TS + (kvh * 8 + lane) * 128 : OOB) - tstart), hi = 0, sum = 0;
    dt = lane < 8 && dt > -400 && dt < 400 ? dt : 0;
#pragma unroll
    for (int k = 0; k < 8; ++k) { const int v = __builtin_amdgcn_readlane(dt, k); hi = v > hi ? v : hi; sum += v; }
    torg = tstart + sum / 8;
    tmax = tstart + hi;
    *(volatile LAS u32*)(L + F_ORG) = torg;
  }
  {
    wait_until(tmax + TH);
    v4u gr[16];
    auto load = [&]() {
#pragma unroll
      for (int i = 0; i < 16; ++i) gr[i] = ld16<SC1>(ws, kvh * 1024 + lane * 16, WS_H + (i >> 1) * 8192 + (i & 1) * 4096);
    };
    load();
    gather<4>(gr, tag, load);
    STAMP(2);
    v4f sa = untag4(gr[0]), ca = untag4(gr[1]);
#pragma unroll
    for (int r = 1; r < 8; ++r) { sa += untag4(gr[2 * r]); ca += untag4(gr[2 * r + 1]); }
    float h[4];
#pragma unroll
    for (int k = 0; k < 4; ++k) h[k] = rbf(xn[k] * rbf(1.f + rbf(ca[k]))) + rbf(sa[k]);
    *(LAS v2u*)(L + L_H + lane * 8) = v2u{pk2(h[0], h[1]), pk2(h[2], h[3])};
    WAIT_LDS();
  }
  lds_wait(L, F_WQ, 3);
  st4(ws, WS_Q + kvh * 16384 + ((j + 1) & 63) * 256 + colb, tagged(gemv_lds(L + L_WT, L + L_H, lane), tag));
  STAMP(3);
  SCHED();
  lds_wait(L, F_WM, 4);
  st4(ws, WS_MP + kvh * 16384 + ((j + 2) & 63) * 256 + colb, tagged(gemv_lds(L + L_WT + 34816, L + L_VEC + kvh * 512, lane), tag));
  STAMP(5);
  SCHED();
  STAMP(26);
  {
    wait_until(tmax + TQ);
    v4u q[16];
    auto load = [&]() {
#pragma unroll
      for (int i = 0; i < 16; ++i) q[i] = ld16<SC1>(ws, kvh * 4096 + lane * 16, WS_Q + (i & 3) * 16384 + (i >> 2) * 1024);
    };
    load();
    gather<4>(q, tag, load);
#pragma unroll
    for (int u = 0; u < 4; ++u) {
      const v4f s = (untag4(q[4 * u]) + untag4(q[4 * u + 1])) + (untag4(q[4 * u + 2]) + untag4(q[4 * u + 3]));
      *(LAS v2u*)(L + L_Q + u * 512 + lane * 8) = v2u{pk2(s[0], s[1]), pk2(s[2], s[3])};
    }
    WAIT_LDS();
    *(volatile LAS u32*)(L + F_Q) = 1u;
    if (b == 0) st4(ws, lane == 0 ? WS_EPOCH : OOB, epoch + 1);
    STAMP(4);
    SCHED();
  }
  if ((i32 & 3) == 2) {
    const int jr = x8 * 8 + (i32 >> 2);
    wait_until(tmax + TMP);
    u32 g[4];
    auto load = [&]() {
#pragma unroll
      for (int i = 0; i < 4; ++i) g[i] = ld4<SC1>(ws, jr * 256 + lane * 4, WS_MP + i * 16384);
    };
    load();
    gather<8>(g, tag, load);
    st4(ws, WS_MR + jr * 256 + lane * 4, bc<u32>(rbf((untag(g[0]) + untag(g[1])) + (untag(g[2]) + untag(g[3])))) | tag);
  }
  if (TABLE) {
    v2f ta[8];
#pragma unroll
    for (int q = 0; q < 8; ++q) ta[q] = v2f{0.f, 0.f};
#pragma nounroll
    for (int i = 0; i < 7; ++i) {
      const LAS u16* m = (const LAS u16*)(L + L_MLP) + i * 512 + lane * 8 + 7;
      const float w1 = bf2f(m[0]), b1 = bf2f(m[FF]), w2 = bf2f(m[2 * FF]);
#pragma unroll
      for (int q = 0; q < 8; ++q) mlp_step(ta[q], tab_input(b, q), w1, b1, w2);
    }
    tab_write(L, ta, 0, lane);
    lds_wait(L, F_TB, 8);
    const float s = sum32(sum16(*(const LAS float*)(L + L_TB + lane * 4) + *(const LAS float*)(L + L_TB + 256 + lane * 4)));
    st4(ws, lane < 16 ? WS_TAB + b * 64 + lane * 4 : OOB, bc<u32>(rbq(rbq(s) + bf2f(b2))));
  }
  STAMP(27);
  SCHED();
  lds_wait(L, F_SM, 7);
  {
    const v2f r = *(const LAS v2f*)(L + L_ML + lane * 8);
    const float mq = lane < 56 ? r.x : -INFINITY, m = untag(bc<u32>(max345(mq))), l = sum345(lane < 56 ? r.y * ex2(mq - m) : 0.f);
    st16(ws, lane < 8 ? WS_ML + (kvh * 64 + j) * 128 + lane * 16 : OOB, v4u{bc<u32>(m) | tag, tagged(l, tag), epoch + 1, epoch + 1});
    if (lane < 8) *(LAS float*)(L + L_BM + lane * 4) = m;
    *(LAS u32*)(L + F_TAG) = tag;
    WAIT_LDS();
    *(volatile LAS u32*)(L + F_BM) = 1u;
  }
  STAMP(6);
  SCHED();
  {
    v4u mm[9];
    auto load = [&]() {
#pragma unroll
      for (int i = 0; i < 8; ++i) mm[i] = ld16<SC1>(ws, lane * 16, WS_MR + 4096 + i * 1024);
      mm[8] = ld16<SC1>(ws, (lane & 8 ? 3 * 4096 + (b >> 3) * 128 : ci * 128) + (lane & 7) * 16, WS_MR);
    };
    load();
    gather<8>(mm, tag, load);
#pragma unroll
    for (int i = 0; i < 8; ++i) *(LAS v4f*)(L + L_MR + i * 1024 + lane * 16) = untag4(mm[i]);
    if (lane < 16) *(LAS v4f*)(L + L_MR + 8192 + lane * 16) = untag4(mm[8]);
  }
  STAMP(7);
  wait_until(torg + T_END - TMLB);
  {
    const u32 warm = TABLE ? ld4<SC1>(ws, lane < 4 ? WS_TAB + (i32 * 4 + lane) * 128 : OOB) : 0u;
    v4u ml[1];
    auto load = [&]() { ml[0] = ld16<SC1>(ws, lane * 128, WS_ML + kvh * 8192 + ((prow >> 2) & 7) * 16); if (ml[0].z != epoch + 1 || ml[0].w != epoch + 1) ml[0].z = ~tag; };
    load();
    gather<8>(ml, tag, load);
    asm volatile("" ::"v"(warm));
    const float m = untag(ml[0].x);
    float M = fmaxf(m, dpp<XOR1>(m)); M = fmaxf(M, dpp<XOR2>(M)); M = max345(fmaxf(M, dpp<XOR7>(M)));
    const float wt = ex2(m - M);
    *(LAS float*)(L + L_RM + lane * 4) = wt / wsum(wt * untag(ml[0].y));
  }
  pace<0>();
  STAMP(8);
  wo[0] = load_wo(p, prow, half, 0, lane, 0);
  issue_wo(p, prow, half, 0, lane, wo, torg);
}

DEV int sums6_slot(int wave, int lane) { return wave * 32 + ((lane >> 5) * 4 + ((lane >> 2) & 2) + ((lane >> 4) & 1)) * 4; }
DEV void sums6_write(LAS unsigned char* rec, v2f v0, v2f v1, v2f v2, v2f v3, v2f v4, v2f v5, int lane) {
  const float a = tr32(v0.x + v0.y, v3.x + v3.y), b = tr32(v1.x + v1.y, v4.x + v4.y), c = tr32(v2.x + v2.y, v5.x + v5.y);
  *(LAS float*)rec = sum421(tr8(tr16(a, b), sum16(c), lane & 8));
}
DEV float sums6_read(const LAS unsigned char* rec, int lane) { return sum16(sum8(*(const LAS float*)(rec + (lane & 31) * 4))) * (1.f / D); }
DEV float inv_sd(float var) { return rcp(rbq(__builtin_amdgcn_sqrtf(rbq(rbq(fmaxf(var, 0.f)) + 1e-5f)))); }

__global__ void __launch_bounds__(512, 2) fused_block(Params p) {
  __shared__ __attribute__((aligned(16))) unsigned char smem[L_END + STAMPS * 2048];
  LAS unsigned char* L = (LAS unsigned char*)smem;
  u32 tstart = STAMPS ? now() : 0;
  const int tid = threadIdx.x, lane = tid & 63, wave = __builtin_amdgcn_readfirstlane(tid >> 6), b = blockIdx.x;
  u32 touch, touch2 = 0;
  {
    u64 pc;
    u32 len;
    asm volatile(".Lfused_begin:\n\ts_getpc_b64 %0\n\ts_mov_b32 %1, .Lfused_end-.Lfused_begin" : "=s"(pc), "=s"(len));
    touch = ld4<0>(mk((const void*)pc, wave == 2 ? len : 0), (((b >> 3) & 15) * 64 + lane) * 128);
  }
  const int x8 = b & 7, i32 = b >> 3;
  const int kvh = x8 >> 1, j = (x8 & 1) * 32 + i32;
  const int prow = kvh * 32 + (j >> 1), half = j & 1;
  const int ci = x8 + 8 * (i32 >> 3);
  const rsrc_t ws = mk(p.ws, WS_END);

  v4u sm[4], mv[3], tile[16], wo[4];
  u32 epoch = 0, b2 = 0;
  int wv = wave;
  asm volatile("" : "+s"(wv));
#pragma unroll
  for (int i = 0; i < 16; ++i) asm volatile("" : "=v"(tile[i]));
#pragma unroll
  for (int i = 0; i < 4; ++i) asm volatile("" : "=v"(sm[i]));
#pragma unroll
  for (int i = 0; i < 3; ++i) asm volatile("" : "=v"(mv[i]));
  if (wv == 0) {
    const rsrc_t rc = mk(p.c, D * 2), rx = mk(p.x, D * 2), rm = mk(p.wmod, 1024 * 6144 * 2);
    sm[0] = ld16<0>(rc, lane * 32);
    sm[1] = ld16<0>(rc, lane * 32 + 16);
    const int vo = (lane >> 3) * 16 * 12288 + (lane & 7) * 16;
    int so = x8 * 128 * 12288 + i32 * 128;
#pragma unroll
    for (int i = 0; i < 16; ++i) { tile[i] = ld16<NT_>(rm, vo, so); so += 12288; asm volatile("" : "+s"(so)); }
    sm[2] = ld16<0>(rx, lane * 16);
    sm[3] = ld16<0>(rx, lane * 16 + 1024);
    epoch = ld4<SC1>(ws, WS_EPOCH);
    b2 = ld2(mk(p.b2, 2), 0);
    SCHED();
    tstart = now();
    if (lane < 8) *(volatile LAS u32*)(L + L_FLAG + lane * 4) = lane == 3 ? tstart : 0u;
    bar();
    st4(ws, (i32 & 19) == 0 && lane < 32 ? WS_TS + ((i32 >> 2) * 8 + x8) * 128 + lane * 4 : OOB, tstart);
  }
  asm volatile("" : "+s"(wv));
  if (wv != 0) {
    if (__builtin_expect(wave == 1 && i32 < 8, 0)) {
      const bool lo4 = i32 < 4, odd = i32 & 1, hi2 = i32 & 2;
      const char* a = (const char*)(lo4 ? (hi2 ? p.wmod : odd ? p.v : p.k) : hi2 ? (odd ? p.w1 : p.ws) : odd ? p.wo : p.wq);
      const u32 off = lo4 ? (hi2 ? (odd ? kvh * 256 : x8 * 128) * 12288 : kvh * 0x1000000) : hi2 ? 0 : kvh * 0x200000;
      const u32 len = lo4 ? (hi2 ? (odd ? 256 : 128) * 12288 : 0x1000000) : hi2 && odd ? 2048 : 0x200000;
      const u32 o = lane * 0x100000;
      touch2 = ld2(mk(a + off, len), o < len ? o : o < len + 0x100000 ? len - 2 : OOB);
      SCHED();
    }
    const bool wq = wv > 4;
    const int ld = wq ? 8192 : 12288;
    const rsrc_t rt = mk(wq ? p.wq : p.wmod, wq ? 1024 * 4096 * 2 : 1024 * 6144 * 2);
    int so = wq ? (kvh * 256 + (wv == 5 ? 0 : wv * 10 - 48)) * 8192 + ((j + 1) & 63) * 128 : (kvh * 256 + (wv - 1) * 8) * 12288 + 4096 + ((j + 2) & 63) * 128;
    const int vo = (lane >> 3) * 32 * ld + (lane & 7) * 16;
    bar();
    tstart = lds_word(L, F_ORG);
    SCHED();
    mv[0] = ld16<0>(mk(p.w1, FF * 2), lane * 16, (wv - 1) * 1024);
    mv[1] = ld16<0>(mk(p.b1, FF * 2), lane * 16, (wv - 1) * 1024);
    mv[2] = ld16<0>(mk(p.w2, FF * 2), lane * 16, (wv - 1) * 1024);
    SCHED();
    if (!wq) wait_until(tstart + TWM);
#pragma unroll
    for (int i = 0; i < 12; ++i) { tile[i] = ld16<NT_>(rt, i < 8 || (i < 10 ? wq : wv == 5) ? vo : OOB, so); so += ld; asm volatile("" : "+s"(so)); }
  }
#pragma unroll
  for (int i = 0; i < 4; ++i) st4(ws, OOB - 64 * i, 0u);
  SCHED();
  if (wave >= 1 && wave <= 4) {
    asm volatile("" ::"v"(touch), "v"(touch2));
    stream_wave<5, 8>(p, tstart, L, b, kvh, j, wave, lane, prow, half, tile, mv, wo);
  } else if (wave > 4) {
    stream_wave<4, 7>(p, tstart, L, b, kvh, j, wave, lane, prow, half, tile, mv, wo);
  } else {
    STAMP(18);
    comm_wave(p, tstart, L, ws, b, x8, i32, kvh, j, lane, prow, half, ci, __builtin_amdgcn_readfirstlane(epoch), tile, sm, (u16)b2, wo);
  }

  PRIO(2);
  bar();
  u32 tp = now();
  const u32 tag = lds_word(L, F_TAG), torg = lds_word(L, F_ORG);
  STAMP(9);
  if (wave < 4) {
    const int line = tid >> 3, e = tid & 7;
    v4f r[7];
#pragma unroll
    for (int k = 0; k < 7; ++k) r[k] = *(const LAS v4f*)(L + L_WV + k * 4096 + line * 128 + ((e ^ (line >> 2)) * 16));
    st16(ws, WS_A + ((kvh * 32 + line) * 64 + j) * 128 + e * 16, cut4(((r[0] + r[1]) + (r[2] + r[3])) + ((r[4] + r[5]) + r[6]), tag));
  }
  PRIO(0);
  u32 wp[2][8];
  int offB = half * 512 + wave * 64 + (lane & 7) * 8 + (lane >> 3), slot = sums6_slot(wave, lane);
  offB = WS_B + ((offB >> 5) * 128 + prow) * 128 + (offB & 31) * 4;
  float wt = *(const LAS float*)(L + L_RM + (tid >> 3) * 4);
  const v4f sb4 = *(const LAS v4f*)(L + L_MR + (tid & 255) * 16), cb4 = *(const LAS v4f*)(L + L_MR + 4096 + (tid & 255) * 16);
  v2f s0 = {sb4[0], sb4[1]}, s1 = {sb4[2], sb4[3]}, c0 = rbf2(v2f{cb4[0], cb4[1]} + 1.f), c1 = rbf2(v2f{cb4[2], cb4[3]} + 1.f);
  float so = *(const LAS float*)(L + L_MR + b * 16 + (tid & 3) * 4), co = rbq(*(const LAS float*)(L + L_MR + 4096 + b * 16 + (tid & 3) * 4) + 1.f);
  float ga = *(const LAS float*)(L + L_MR + 8192 + (tid & 31) * 4), gb = *(const LAS float*)(L + L_MR + 8192 + 128 + (b & 7) * 16 + (tid & 3) * 4);
  float xa = bf2f(*(const LAS u16*)(L + L_MLP + 3 * 7168 + (ci * 32 + (tid & 31)) * 2));
  if (wave < 4) sums6_write(L + L_S2 + slot, c0 + c1, c0 * c0 + c1 * c1, c0 * s0 + c1 * s1, s0 + s1, s0 * s0 + s1 * s1, s0, lane);
  asm volatile("" : "+v"(offB), "+v"(slot), "+v"(wt), "+v"(s0), "+v"(s1), "+v"(c0), "+v"(c1), "+v"(so), "+v"(co), "+v"(ga), "+v"(gb), "+v"(xa));
  {
    v4u v[1];
    auto load = [&]() { v[0] = ld16<SC1>(ws, tid * 16, WS_A + prow * 8192); };
    wait_until(tp + DLA);
    wait_until(torg + T_END + TA);
    load();
    SCHED();
#pragma unroll
    for (int i = 0; i < 2; ++i)
#pragma unroll
      for (int k = 0; k < 4; ++k) {
        wp[i][2 * k] = __builtin_amdgcn_perm(wo[2 * i + 1][k], wo[2 * i][k], prow & 1 ? 0x01000504u : 0x05040100u);
        wp[i][2 * k + 1] = __builtin_amdgcn_perm(wo[2 * i + 1][k], wo[2 * i][k], prow & 1 ? 0x03020706u : 0x07060302u);
        asm volatile("" : "+v"(wp[i][2 * k]), "+v"(wp[i][2 * k + 1]));
      }
    PRIO(2);
    gather<0>(v, tag, load);
    const v4f s = untag4(v[0]) * wt;
    const float r = sum8(tr16(tr32(s[0], s[2]), tr32(s[1], s[3])));
    if (!(lane & 8)) *(LAS float*)(L + L_RA + wave * 128 + (lane & 7) * 16 + (lane >> 4) * 4) = r;
  }
  bar();
  STAMP(10);
  {
    v4f r8[8];
#pragma unroll
    for (int k = 0; k < 8; ++k) r8[k] = *(const LAS v4f*)(L + L_RA + k * 128 + (lane >> 3) * 16);
    const v4f s = ((r8[0] + r8[1]) + (r8[2] + r8[3])) + ((r8[4] + r8[5]) + (r8[6] + r8[7]));
    const u32 o01 = pk2(s[0], s[1]), o23 = pk2(s[2], s[3]);
    float a[8];
#pragma unroll
    for (int k = 0; k < 8; ++k) a[k] = dot2(wp[1][k], o23, dot2(wp[0][k], o01, 0.f));
    st4(ws, offB, cut(tr8cols(a, lane), tag));
  }
  tp = now();
  STAMP(11);
  PRIO(0);
  float kc, kcc, kcs, ks, kss;
  {
    const float k = sums6_read(L + L_S2, lane);
    kc = lane_of(k, 0); kcc = lane_of(k, 1); kcs = lane_of(k, 2); ks = lane_of(k, 4); kss = lane_of(k, 5);
    asm volatile("" : "+v"(kc), "+v"(kcc), "+v"(kcs), "+v"(ks), "+v"(kss));
  }
  PRIO(2);
  if ((i32 & 7) == 0) {
    v4u v[2];
    auto load = [&]() { v[0] = ld16<SC1>(ws, tid * 16, WS_B + ci * 16384); v[1] = ld16<SC1>(ws, tid * 16, WS_B + ci * 16384 + 8192); };
    wait_until(tp + DLB);
    load();
    gather<0>(v, tag, load);
    const v4f s = untag4(v[0]) + untag4(v[1]);
    const float r = sum8(tr16(tr32(s[0], s[2]), tr32(s[1], s[3])));
    if (!(lane & 8)) *(LAS float*)(L + L_RB + wave * 128 + (lane & 7) * 16 + (lane >> 4) * 4) = r;
    bar();
    if (wave == 0) {
      const LAS float* a = (const LAS float*)(L + L_RB + (lane >> 5) * 512 + (lane & 31) * 4);
      const float x1 = rbq(xa + rbq(ga * rbq(sum32((a[0] + a[32]) + (a[64] + a[96])))));
      st4(ws, lane < 32 ? WS_C + ci * 128 + lane * 4 : OOB, bc<u32>(x1) | tag);
    }
  }
  STAMP(12);
  if (wave < 4) {
    v4u v[1];
    auto load = [&]() { v[0] = ld16<SC1>(ws, tid * 16, WS_C); };
    wait_until(tp + DLC);
    load();
    gather<0>(v, tag, load);
    const v4f xs = untag4(v[0]);
    const v2f x0 = {xs[0], xs[1]}, x1 = {xs[2], xs[3]}, y0 = x0 * c0, y1 = x1 * c1;
    STAMP(24);
    if (tid == b) *(LAS v4f*)(L + L_X) = xs;
    sums6_write(L + L_S1 + slot, x0 + x1, x0 * x0 + x1 * x1, y0 + y1, y0 * y0 + y1 * y1, y0 * c0 + y1 * c1, y0 * s0 + y1 * s1, lane);
  }
  if (TABLE && wave != 0) PRIO(0);
  bar();
  STAMP(13);
  const float xo = *(const LAS float*)(L + L_X + (lane & 3) * 4);
  float t;
  {
    const float k = sums6_read(L + L_S1, lane);
    const float mean = lane_of(k, 0), mb = rbq(mean), r = inv_sd(fmaf(-mean, mean, lane_of(k, 1)));
    const float A = fmaf(-mb, kc, lane_of(k, 2)), B = fmaf(-mb, fmaf(-mb, kcc, 2.f * lane_of(k, 5)), lane_of(k, 4)), C = fmaf(-mb, kcs, lane_of(k, 6));
    const float m2 = fmaf(r, A, ks), q2 = fmaf(r, fmaf(r, B, 2.f * C), kss);
    const float h = rbq(rbq(rbq(rbq(xo - mb) * r) * co) + so);
    t = rbq(rbq(h - rbq(m2)) * inv_sd(fmaf(-m2, m2, q2)));
  }
  STAMP(25);
  const u32 ta = ((bc<u32>(t) >> 16) & 0x7fffu) - (TEXP << 7), ti = t == 0.f ? 0u : ta + (bc<u32>(t) >> 31 << 11);
  float y;
  if (TABLE && __all(t == 0.f || (ta < 2048u && ti != 0u))) {
    if (wave != 0) goto finished;
    y = bc<float>(ld4<SC1>(ws, ti * 4, WS_TAB));
  } else {
    const v2f t0 = {dpp<0x00>(t), dpp<0x55>(t)}, t1 = {dpp<0xAA>(t), dpp<0xFF>(t)};
    v2f acc0 = {0.f, 0.f}, acc1 = {0.f, 0.f};
#pragma nounroll
    for (int i = 0; i < 7; ++i) {
      const LAS u16* m = (const LAS u16*)(L + L_MLP) + wave * 448 + i * 64 + lane;
      const float w1 = bf2f(m[0]), b1 = bf2f(m[FF]), w2 = bf2f(m[2 * FF]);
      mlp_step(acc0, t0, w1, b1, w2);
      mlp_step(acc1, t1, w1, b1, w2);
    }
    const float s = sum421(sum8(tr16(tr32(acc0.x, acc1.x), tr32(acc0.y, acc1.y))));
    *(LAS float*)(L + L_RF + wave * 16 + (lane >> 4) * 4) = s;
    bar();
    if (wave != 0) goto finished;
    const float a = sum8(sum16(*(const LAS float*)(L + L_RF + (lane & 31) * 4)));
    y = rbq(rbq(a + dpp<ROR4>(a)) + bf2f(*(const LAS u16*)(L + L_MLP + 3 * 7168 + 2048)));
  }
  STAMP(14);
  y = xo + rbq(gb * y);
  if (lane < 4) ((u16*)p.out)[b * 4 + lane] = (u16)pk2(y, y);
finished:
  asm volatile(".Lfused_end:");
}

extern "C" void kernel_launch(void* const* d_in, const int* in_sizes, int n_in, void* d_out, int out_size, void* d_ws, size_t ws_size, hipStream_t stream) {
  Params p;
  p.x = d_in[0]; p.k = d_in[1]; p.v = d_in[2]; p.wq = d_in[3]; p.wo = d_in[4];
  p.w1 = d_in[6]; p.b1 = d_in[7]; p.w2 = d_in[8]; p.b2 = d_in[9]; p.c = d_in[10]; p.wmod = d_in[11];
  p.out = d_out; p.ws = d_ws;
  hipLaunchKernelGGL(fused_block, dim3(256), dim3(512), 0, stream, p);
}
```
